```python
import torch

def _softmax(x, axis=-1):
    return torch.softmax(x, dim=axis)
def _gelu(x):
    return torch.nn.functional.gelu(x, approximate="tanh")
def _silu(x):
    return torch.nn.functional.silu(x)
def _sigmoid(x):
    return torch.sigmoid(x)
def _layernorm(x, axis=-1, eps=1e-5):
    m = x.mean(dim=axis, keepdim=True)
    v = x.var(dim=axis, keepdim=True, unbiased=False)
    return (x - m) / torch.sqrt(v + eps)
def _scatter_add(tgt, idx, src, axis=0):
    return tgt.clone().index_add_(axis, idx, src)
def _segment_sum(src, seg, n):
    out = torch.zeros((n,) + src.shape[1:], dtype=src.dtype)
    return out.index_add_(0, seg, src)
def _pad(x, pw):
    flat = tuple(v for pair in reversed(pw) for v in pair)
    return torch.nn.functional.pad(x, flat)

kernel_name = 'standalone__mha_block_cached__near_ridge'

def setup_inputs():

    _w = (lambda a: (lambda t: t.to(torch.bfloat16) if t.is_floating_point() else t)(torch.as_tensor(a)))
    B, Sq, D = 32, 144, 512
    H, Sk, Dh = 32, 512, 128
    return {
        "x": _w((lambda *s: torch.randn(*s, dtype=torch.bfloat16) * 0.8)(B, Sq, D)),
        "k_cache": _w((lambda *s: torch.randn(*s, dtype=torch.bfloat16) * 0.8)(B, H, Sk, Dh)),
        "v_cache": _w((lambda *s: torch.randn(*s, dtype=torch.bfloat16) * 0.05)(B, H, Sk, Dh)),
        "wq": _w((lambda *s: torch.randn(*s, dtype=torch.bfloat16) * 0.8)(D, H * Dh) * (D ** -0.5)),
        "wo": _w((lambda *s: torch.randn(*s, dtype=torch.bfloat16) * 0.05)(H * Dh, D) * ((H * Dh) ** -0.5)),
    }

def reference(x, k_cache, v_cache, wq, wo):
    B, Sq, D = x.shape
    H, Dh = 32, 128
    q = (lambda x, *d: x.permute(*d))((x @ wq).reshape(B, Sq, H, Dh), 0, 2, 1, 3)
    scores = torch.einsum("bhqd,bhkd->bhqk", q, k_cache) * (Dh ** -0.5)
    p = _softmax(scores, axis=-1)
    o = torch.einsum("bhqk,bhkd->bhqd", p, v_cache)
    return (lambda x, *d: x.permute(*d))(o, 0, 2, 1, 3).reshape(B, Sq, H * Dh) @ wo
```

```cpp
#include <hip/hip_runtime.h>
#include <hip/hip_bf16.h>

typedef __attribute__((ext_vector_type(8))) short bf16x8;
typedef __attribute__((ext_vector_type(4))) short s16x4;
typedef __attribute__((ext_vector_type(4))) float f32x4;
typedef __attribute__((ext_vector_type(2))) unsigned u32x2;
typedef __attribute__((ext_vector_type(4))) unsigned u32x4;
typedef __attribute__((address_space(3))) s16x4* lds_s16x4_ptr;
typedef __attribute__((address_space(3))) void* lds_void_ptr;

constexpr int NB = 32, SQ = 144, DM = 512, NH = 32, SK = 512, DH = 128;
constexpr int HD = NH * DH;

#define MFMA16(a, b, c) __builtin_amdgcn_mfma_f32_16x16x32_bf16((a), (b), (c), 0, 0, 0)

__device__ __forceinline__ unsigned pack_bf16(float lo, float hi) {
  __hip_bfloat16 a = __float2bfloat16(lo), b = __float2bfloat16(hi);
  unsigned short ua = *reinterpret_cast<unsigned short*>(&a), ub = *reinterpret_cast<unsigned short*>(&b);
  return (unsigned)ua | ((unsigned)ub << 16);
}
__device__ __forceinline__ bf16x8 pack8(const f32x4& a, const f32x4& b) {
  u32x4 w = {pack_bf16(a[0], a[1]), pack_bf16(a[2], a[3]), pack_bf16(b[0], b[1]), pack_bf16(b[2], b[3])};
  return __builtin_bit_cast(bf16x8, w);
}
__device__ __forceinline__ s16x4 tr_read(const char* smem, int byte_off) {
  return __builtin_amdgcn_ds_read_tr16_b64_v4i16((lds_s16x4_ptr)(smem + byte_off));
}
__device__ __forceinline__ bf16x8 cat8(const s16x4& lo, const s16x4& hi) {
  return __builtin_shufflevector(lo, hi, 0, 1, 2, 3, 4, 5, 6, 7);
}
__device__ __forceinline__ void glds16(const void* gsrc, char* lds_dst) {
  __builtin_amdgcn_global_load_lds(gsrc, (lds_void_ptr)lds_dst, 16, 0, 0);
}
typedef __amdgpu_buffer_rsrc_t rsrc_t;
__device__ __forceinline__ rsrc_t make_rsrc(const void* base, unsigned bytes) {
  return __builtin_amdgcn_make_buffer_rsrc(const_cast<void*>(base), (short)0, bytes, 0x00020000);
}
__device__ __forceinline__ void blds16(rsrc_t r, unsigned voff, unsigned soff, char* lds_dst) {
  __builtin_amdgcn_raw_ptr_buffer_load_lds(r, (lds_void_ptr)lds_dst, 16, voff, soff, 0, 0);
}

constexpr int TILE_BYTES = 64 * 256;

__global__ __launch_bounds__(192, 2) void attn_fused_kernel(const __hip_bfloat16* __restrict__ x,
                                                            const __hip_bfloat16* __restrict__ kc,
                                                            const __hip_bfloat16* __restrict__ vc,
                                                            const __hip_bfloat16* __restrict__ wq,
                                                            __hip_bfloat16* __restrict__ obuf) {
  __shared__ __attribute__((aligned(16))) char smem[4 * TILE_BYTES];
  const int tid = threadIdx.x;
  const int w = __builtin_amdgcn_readfirstlane(tid >> 6);
  const int lane = tid & 63;
  const int g = lane >> 4, l15 = lane & 15, q4 = l15 >> 2, p4 = lane & 3;
  const int bh = blockIdx.x, b = bh >> 5, h = bh & 31;

  const __hip_bfloat16* kbase = kc + (size_t)bh * SK * DH;
  const __hip_bfloat16* vbase = vc + (size_t)bh * SK * DH;
  const __hip_bfloat16* wbase = wq + h * DH;
  const __hip_bfloat16* xbase = x + ((size_t)b * SQ + 48 * w + l15) * DM + 8 * g;

  const int srow = lane >> 4, sslot = lane & 15;

  const rsrc_t krs = make_rsrc(kbase, SK * DH * 2), vrs = make_rsrc(vbase, SK * DH * 2);
  const rsrc_t wrs = make_rsrc(wbase, (DM - 1) * HD * 2 + DH * 2);
  const unsigned kvo_e = srow * 256 + 16 * (sslot ^ (srow << 1));
  const unsigned kvo_o = srow * 256 + 16 * (sslot ^ ((4 + srow) << 1));
  auto stage_kv = [&](int j, char* kbuf, char* vbuf) {
#pragma unroll
    for (int n = 0; n < 11; ++n) {
      int pc = w + 3 * n;
      pc = pc > 31 ? 31 : pc;
      const int isv = pc >> 4, pp = pc & 15;
      const unsigned vo = (pp & 1) ? kvo_o : kvo_e;
      blds16(isv ? vrs : krs, vo, (64 * j + 4 * pp) * 256, (isv ? vbuf : kbuf) + pp * 1024);
    }
  };
  const unsigned wo_l = srow * (HD * 2) + 16 * (sslot ^ (srow << 2));
  auto stage_w = [&](int step, char* buf) {
#pragma unroll
    for (int n = 0; n < 6; ++n) {
      int pc = w + 3 * n;
      pc = pc > 15 ? 15 : pc;
      blds16(wrs, wo_l, (64 * step + 4 * pc) * (HD * 2), buf + pc * 1024);
    }
  };

  char* buf0 = smem;
  char* buf1 = smem + TILE_BYTES;
  char* buf2 = smem + 2 * TILE_BYTES;
  char* buf3 = smem + 3 * TILE_BYTES;

  stage_w(0, buf2);
  stage_kv(0, buf0, buf1);

  f32x4 qacc[3][8];
#pragma unroll
  for (int i = 0; i < 3; ++i)
#pragma unroll
    for (int c = 0; c < 8; ++c) qacc[i][c] = f32x4{0.f, 0.f, 0.f, 0.f};

  int woff[4];
#pragma unroll
  for (int u = 0; u < 4; ++u) woff[u] = 256 * (8 * g + q4) + 64 * (u ^ q4) + 16 * p4;

  bf16x8 xf[3][2];
#pragma unroll
  for (int i = 0; i < 3; ++i)
#pragma unroll
    for (int s = 0; s < 2; ++s) xf[i][s] = *reinterpret_cast<const bf16x8*>(xbase + (size_t)16 * i * DM + 32 * s);

#pragma unroll 1
  for (int step = 0; step < 8; ++step) {
    __syncthreads();
    char* wcur = (step & 1) ? buf3 : buf2;
    char* wnext = (step & 1) ? buf2 : buf3;
    bf16x8 xn[3][2];
    if (step + 1 < 8) {
      stage_w(step + 1, wnext);
#pragma unroll
      for (int i = 0; i < 3; ++i)
#pragma unroll
        for (int s = 0; s < 2; ++s)
          xn[i][s] = *reinterpret_cast<const bf16x8*>(xbase + (size_t)16 * i * DM + 64 * (step + 1) + 32 * s);
    }
#pragma unroll
    for (int s = 0; s < 2; ++s) {
#pragma unroll
      for (int u = 0; u < 4; ++u) {
#pragma unroll
        for (int t = 0; t < 2; ++t) {
          const s16x4 lo = tr_read(wcur, woff[u] + 8192 * s + 8 * t);
          const s16x4 hi = tr_read(wcur, woff[u] + 8192 * s + 1024 + 8 * t);
          const bf16x8 a = cat8(lo, hi);
#pragma unroll
          for (int i = 0; i < 3; ++i) qacc[i][2 * u + t] = MFMA16(a, xf[i][s], qacc[i][2 * u + t]);
        }
      }
    }
    if (step + 1 < 8) {
#pragma unroll
      for (int i = 0; i < 3; ++i)
#pragma unroll
        for (int s = 0; s < 2; ++s) xf[i][s] = xn[i][s];
    }
  }

  bf16x8 qf[3][4];
#pragma unroll
  for (int i = 0; i < 3; ++i)
#pragma unroll
    for (int u = 0; u < 4; ++u) qf[i][u] = pack8(qacc[i][2 * u], qacc[i][2 * u + 1]);

  f32x4 oacc[3][8];
#pragma unroll
  for (int i = 0; i < 3; ++i)
#pragma unroll
    for (int c = 0; c < 8; ++c) oacc[i][c] = f32x4{0.f, 0.f, 0.f, 0.f};
  float mrow[3], lsum[3];
#pragma unroll
  for (int i = 0; i < 3; ++i) { mrow[i] = -__builtin_inff(); lsum[i] = 0.f; }

  int koff[4];
  {
    const int sw = (l15 & 7) << 1;
#pragma unroll
    for (int u = 0; u < 4; ++u) koff[u] = 256 * l15 + 16 * ((4 * u + g) ^ sw);
  }
  int voff[8];
  {
    const int m = 4 * (g & 1) + q4;
#pragma unroll
    for (int c = 0; c < 8; ++c) voff[c] = 256 * (4 * g + q4) + 32 * (c ^ m) + 8 * p4;
  }
  constexpr float CSC = 0.08838834764831845f * 1.4426950408889634f;

#pragma unroll 1
  for (int j = 0; j < 8; ++j) {
    __syncthreads();
    char* kb = (j & 1) ? buf2 : buf0;
    char* vb = (j & 1) ? buf3 : buf1;
    if (j + 1 < 8) stage_kv(j + 1, (j & 1) ? buf0 : buf2, (j & 1) ? buf1 : buf3);

    f32x4 sacc[4][3];
#pragma unroll
    for (int kt = 0; kt < 4; ++kt)
#pragma unroll
      for (int i = 0; i < 3; ++i) sacc[kt][i] = f32x4{0.f, 0.f, 0.f, 0.f};
#pragma unroll
    for (int kt = 0; kt < 4; ++kt) {
#pragma unroll
      for (int u = 0; u < 4; ++u) {
        const bf16x8 a = *reinterpret_cast<const bf16x8*>(kb + koff[u] + 4096 * kt);
#pragma unroll
        for (int i = 0; i < 3; ++i) sacc[kt][i] = MFMA16(a, qf[i][u], sacc[kt][i]);
      }
    }
    bf16x8 pf[3][2];
#pragma unroll
    for (int i = 0; i < 3; ++i) {
      float mx = sacc[0][i][0];
#pragma unroll
      for (int kt = 0; kt < 4; ++kt)
#pragma unroll
        for (int r = 0; r < 4; ++r) mx = fmaxf(mx, sacc[kt][i][r]);
      mx = fmaxf(mx, __shfl_xor(mx, 16));
      mx = fmaxf(mx, __shfl_xor(mx, 32));
      const float mnew = fmaxf(mrow[i], mx);
      const float alpha = __builtin_amdgcn_exp2f((mrow[i] - mnew) * CSC);
      mrow[i] = mnew;
      const float mc = -mnew * CSC;
      float ps = 0.f;
#pragma unroll
      for (int kt = 0; kt < 4; ++kt)
#pragma unroll
        for (int r = 0; r < 4; ++r) {
          const float pv = __builtin_amdgcn_exp2f(fmaf(sacc[kt][i][r], CSC, mc));
          sacc[kt][i][r] = pv;
          ps += pv;
        }
      lsum[i] = lsum[i] * alpha + ps;
#pragma unroll
      for (int c = 0; c < 8; ++c) oacc[i][c] *= alpha;
      pf[i][0] = pack8(sacc[0][i], sacc[1][i]);
      pf[i][1] = pack8(sacc[2][i], sacc[3][i]);
    }
#pragma unroll
    for (int ks = 0; ks < 2; ++ks) {
#pragma unroll
      for (int c = 0; c < 8; ++c) {
        const s16x4 lo = tr_read(vb, voff[c] + 8192 * ks);
        const s16x4 hi = tr_read(vb, voff[c] + 8192 * ks + 4096);
        const bf16x8 a = cat8(lo, hi);
#pragma unroll
        for (int i = 0; i < 3; ++i) oacc[i][c] = MFMA16(a, pf[i][ks], oacc[i][c]);
      }
    }
  }

  int lane2 = __builtin_amdgcn_mbcnt_hi(~0u, __builtin_amdgcn_mbcnt_lo(~0u, 0u));
  asm volatile("" : "+v"(lane2));
#pragma unroll
  for (int i = 0; i < 3; ++i) {
    float l = lsum[i];
    l += __shfl_xor(l, 16);
    l += __shfl_xor(l, 32);
    const float inv = 1.0f / l;
    __hip_bfloat16* orow = obuf + ((size_t)b * SQ + 48 * w + 16 * i + (lane2 & 15)) * HD + h * DH + 4 * (lane2 >> 4);
#pragma unroll
    for (int c = 0; c < 8; ++c) {
      u32x2 v = {pack_bf16(oacc[i][c][0] * inv, oacc[i][c][1] * inv), pack_bf16(oacc[i][c][2] * inv, oacc[i][c][3] * inv)};
      *reinterpret_cast<u32x2*>(orow + 16 * c) = v;
    }
  }
}

constexpr int WT_BYTES = 64 * 128;

__global__ __launch_bounds__(192, 2) void out_proj_kernel(const __hip_bfloat16* __restrict__ obuf,
                                                          const __hip_bfloat16* __restrict__ wo,
                                                          __hip_bfloat16* __restrict__ out) {
  __shared__ __attribute__((aligned(16))) char smem[2 * WT_BYTES];
  const int tid = threadIdx.x;
  const int w = __builtin_amdgcn_readfirstlane(tid >> 6);
  const int lane = tid & 63;
  const int g = lane >> 4, l15 = lane & 15, q4 = l15 >> 2, p4 = lane & 3;
  const int b = blockIdx.x >> 3, n0 = (blockIdx.x & 7) * 64;

  const __hip_bfloat16* abase = obuf + ((size_t)b * SQ + 48 * w + l15) * HD + 8 * g;
  const int srow = lane >> 3, sslot = lane & 7;
  auto f2 = [](int row) { return ((row >> 1) & 1) | (((row >> 3) & 1) << 1); };
  auto stage = [&](int step, char* buf) {
#pragma unroll
    for (int n = 0; n < 3; ++n) {
      int pc = w + 3 * n;
      pc = pc > 7 ? 7 : pc;
      const int row = 8 * pc + srow;
      const int ch = sslot ^ (f2(row) << 1);
      const __hip_bfloat16* src = wo + (size_t)(64 * step + row) * DM + n0 + ch * 8;
      glds16(src, buf + pc * 1024);
    }
  };
  int aoff[2][4];
#pragma unroll
  for (int e = 0; e < 2; ++e)
#pragma unroll
    for (int c = 0; c < 4; ++c) {
      const int row = 8 * g + 4 * e + q4;
      aoff[e][c] = 128 * row + 32 * (c ^ f2(row)) + 8 * p4;
    }

  f32x4 acc[3][4];
#pragma unroll
  for (int i = 0; i < 3; ++i)
#pragma unroll
    for (int c = 0; c < 4; ++c) acc[i][c] = f32x4{0.f, 0.f, 0.f, 0.f};

  stage(0, smem);
  bf16x8 bf[3][2];
#pragma unroll
  for (int i = 0; i < 3; ++i)
#pragma unroll
    for (int s = 0; s < 2; ++s) bf[i][s] = *reinterpret_cast<const bf16x8*>(abase + (size_t)16 * i * HD + 32 * s);

#pragma unroll 1
  for (int step = 0; step < 64; ++step) {
    __syncthreads();
    char* cur = smem + (step & 1) * WT_BYTES;
    char* nxt = smem + ((step + 1) & 1) * WT_BYTES;
    bf16x8 bn[3][2];
    if (step + 1 < 64) {
      stage(step + 1, nxt);
#pragma unroll
      for (int i = 0; i < 3; ++i)
#pragma unroll
        for (int s = 0; s < 2; ++s)
          bn[i][s] = *reinterpret_cast<const bf16x8*>(abase + (size_t)16 * i * HD + 64 * (step + 1) + 32 * s);
    }
#pragma unroll
    for (int s = 0; s < 2; ++s) {
#pragma unroll
      for (int c = 0; c < 4; ++c) {
        const s16x4 lo = tr_read(cur, aoff[0][c] + 4096 * s);
        const s16x4 hi = tr_read(cur, aoff[1][c] + 4096 * s);
        const bf16x8 a = cat8(lo, hi);
#pragma unroll
        for (int i = 0; i < 3; ++i) acc[i][c] = MFMA16(a, bf[i][s], acc[i][c]);
      }
    }
    if (step + 1 < 64) {
#pragma unroll
      for (int i = 0; i < 3; ++i)
#pragma unroll
        for (int s = 0; s < 2; ++s) bf[i][s] = bn[i][s];
    }
  }
#pragma unroll
  for (int i = 0; i < 3; ++i) {
    __hip_bfloat16* orow = out + ((size_t)b * SQ + 48 * w + 16 * i + l15) * DM + n0 + 4 * g;
#pragma unroll
    for (int c = 0; c < 4; ++c) {
      u32x2 v = {pack_bf16(acc[i][c][0], acc[i][c][1]), pack_bf16(acc[i][c][2], acc[i][c][3])};
      *reinterpret_cast<u32x2*>(orow + 16 * c) = v;
    }
  }
}

extern "C" void kernel_launch(void* const* d_in, const int* in_sizes, int n_in, void* d_out, int out_size,
                              void* d_ws, size_t ws_size, hipStream_t stream) {
  const __hip_bfloat16* x = (const __hip_bfloat16*)d_in[0];
  const __hip_bfloat16* kc = (const __hip_bfloat16*)d_in[1];
  const __hip_bfloat16* vc = (const __hip_bfloat16*)d_in[2];
  const __hip_bfloat16* wq = (const __hip_bfloat16*)d_in[3];
  const __hip_bfloat16* wo = (const __hip_bfloat16*)d_in[4];
  __hip_bfloat16* out = (__hip_bfloat16*)d_out;
  __hip_bfloat16* obuf = (__hip_bfloat16*)d_ws;
  attn_fused_kernel<<<NB * NH, 192, 0, stream>>>(x, kc, vc, wq, obuf);
  out_proj_kernel<<<NB * 8, 192, 0, stream>>>(obuf, wo, out);
}
```

```cpp
#include <hip/hip_runtime.h>
#include <hip/hip_bf16.h>

typedef __attribute__((ext_vector_type(8))) short bf16x8;
typedef __attribute__((ext_vector_type(4))) short s16x4;
typedef __attribute__((ext_vector_type(4))) float f32x4;
typedef __attribute__((ext_vector_type(2))) unsigned u32x2;
typedef __attribute__((ext_vector_type(4))) unsigned u32x4;
typedef __attribute__((address_space(3))) s16x4* lds_s16x4_ptr;
typedef __attribute__((address_space(3))) void* lds_void_ptr;

constexpr int NB = 32, SQ = 144, DM = 512, NH = 32, SK = 512, DH = 128;
constexpr int HD = NH * DH;

#define MFMA16(a, b, c) __builtin_amdgcn_mfma_f32_16x16x32_bf16((a), (b), (c), 0, 0, 0)

__device__ __forceinline__ unsigned pack_bf16(float lo, float hi) {
  unsigned r;
  asm("v_cvt_pk_bf16_f32 %0, %1, %2" : "=v"(r) : "v"(lo), "v"(hi));
  return r;
}
__device__ __forceinline__ float grp_max(float x) {
  auto r = __builtin_amdgcn_permlane16_swap(__float_as_uint(x), __float_as_uint(x), false, false);
  x = fmaxf(__uint_as_float(r[0]), __uint_as_float(r[1]));
  auto q = __builtin_amdgcn_permlane32_swap(__float_as_uint(x), __float_as_uint(x), false, false);
  return fmaxf(__uint_as_float(q[0]), __uint_as_float(q[1]));
}
__device__ __forceinline__ float grp_sum(float x) {
  auto r = __builtin_amdgcn_permlane16_swap(__float_as_uint(x), __float_as_uint(x), false, false);
  x = __uint_as_float(r[0]) + __uint_as_float(r[1]);
  auto q = __builtin_amdgcn_permlane32_swap(__float_as_uint(x), __float_as_uint(x), false, false);
  return __uint_as_float(q[0]) + __uint_as_float(q[1]);
}
__device__ __forceinline__ unsigned pack_bf16_c(float lo, float hi) {
  __hip_bfloat16 a = __float2bfloat16(lo), b = __float2bfloat16(hi);
  unsigned short ua = *reinterpret_cast<unsigned short*>(&a), ub = *reinterpret_cast<unsigned short*>(&b);
  return (unsigned)ua | ((unsigned)ub << 16);
}
__device__ __forceinline__ bf16x8 pack8_c(const f32x4& a, const f32x4& b) {
  u32x4 w = {pack_bf16_c(a[0], a[1]), pack_bf16_c(a[2], a[3]), pack_bf16_c(b[0], b[1]), pack_bf16_c(b[2], b[3])};
  return __builtin_bit_cast(bf16x8, w);
}
__device__ __forceinline__ bf16x8 pack8(const f32x4& a, const f32x4& b) {
  u32x4 w = {pack_bf16(a[0], a[1]), pack_bf16(a[2], a[3]), pack_bf16(b[0], b[1]), pack_bf16(b[2], b[3])};
  return __builtin_bit_cast(bf16x8, w);
}
__device__ __forceinline__ s16x4 tr_read(const char* smem, int byte_off) {
  return __builtin_amdgcn_ds_read_tr16_b64_v4i16((lds_s16x4_ptr)(smem + byte_off));
}
__device__ __forceinline__ bf16x8 cat8(const s16x4& lo, const s16x4& hi) {
  return __builtin_shufflevector(lo, hi, 0, 1, 2, 3, 4, 5, 6, 7);
}
__device__ __forceinline__ void glds16(const void* gsrc, char* lds_dst) {
  __builtin_amdgcn_global_load_lds(gsrc, (lds_void_ptr)lds_dst, 16, 0, 0);
}
typedef __amdgpu_buffer_rsrc_t rsrc_t;
__device__ __forceinline__ rsrc_t make_rsrc(const void* base, unsigned bytes) {
  return __builtin_amdgcn_make_buffer_rsrc(const_cast<void*>(base), (short)0, bytes, 0x00020000);
}
__device__ __forceinline__ void blds16(rsrc_t r, unsigned voff, unsigned soff, char* lds_dst) {
  __builtin_amdgcn_raw_ptr_buffer_load_lds(r, (lds_void_ptr)lds_dst, 16, voff, soff, 0, 0);
}

constexpr int TILE_BYTES = 64 * 256;

__device__ __forceinline__ u32x4 make_rs(const void* base, unsigned bytes) {
  const unsigned long long a = (unsigned long long)base;
  u32x4 r = {(unsigned)a, (unsigned)(a >> 32) & 0xffffu, bytes, 0x00020000u};
  return r;
}
__device__ __forceinline__ void dma16(const u32x4& rs, unsigned voff, unsigned soff, unsigned lds_addr) {
  asm volatile("s_mov_b32 m0, %0\n\ts_nop 4\n\tbuffer_load_dwordx4 %1, %2, %3 offen lds"
               :: "s"(__builtin_amdgcn_readfirstlane(lds_addr)), "v"(voff), "s"(rs), "s"(__builtin_amdgcn_readfirstlane(soff)) : "memory");
}
__device__ __forceinline__ void dma16_nt(const u32x4& rs, unsigned voff, unsigned soff, unsigned lds_addr) {
  asm volatile("s_mov_b32 m0, %0\n\ts_nop 4\n\tbuffer_load_dwordx4 %1, %2, %3 offen nt lds"
               :: "s"(__builtin_amdgcn_readfirstlane(lds_addr)), "v"(voff), "s"(rs), "s"(__builtin_amdgcn_readfirstlane(soff)) : "memory");
}
#define SB() __builtin_amdgcn_sched_barrier(0)
#define WAIT_VM(n) asm volatile("s_waitcnt vmcnt(" #n ")" ::: "memory")
typedef __attribute__((address_space(1))) unsigned gu32;

constexpr int GSLOT = 18 * 1024 + 2 * TILE_BYTES;

template <int MODE>
__global__ __launch_bounds__(512, 1) void proj_kernel(const void* __restrict__ act, const __hip_bfloat16* __restrict__ wgt,
                                                      void* __restrict__ dst, float* __restrict__ part,
                                                      unsigned* __restrict__ flags) {
  __shared__ __attribute__((aligned(16))) char smem[3 * GSLOT + 16];
  const int tid = threadIdx.x;
  const int w = __builtin_amdgcn_readfirstlane(tid >> 6);
  const int chf = w >> 2, u = w & 3;
  const int lane = tid & 63;
  const int g = lane >> 4, l15 = lane & 15, q4 = l15 >> 2, p4 = lane & 3;
  const int srow = lane >> 4, sslot = lane & 15;
  const unsigned lds0 = (unsigned)(size_t)(__attribute__((address_space(3))) char*)smem;
  constexpr int NSTEP = MODE == 0 ? 8 : 16;
  constexpr int WLD = MODE == 0 ? HD * 2 : DM * 2;

  const int idx = blockIdx.x >> 3;
  int b, sel, kq = 0;
  u32x4 ars, wrs;
  unsigned avo;
  if (MODE == 0) {
    if (blockIdx.x == 0 && tid < 256) flags[tid] = 0;
    b = (blockIdx.x & 7) * 4 + (idx >> 4);
    sel = idx & 15;
    ars = make_rs((const char*)act + (size_t)b * SQ * DM * 2, SQ * DM * 2);
    wrs = make_rs(wgt + sel * 256, (DM - 1) * HD * 2 + 512);
    avo = l15 * (DM * 2) + g * 16;
  } else {
    b = (blockIdx.x & 7) * 4 + (idx >> 3);
    sel = (idx >> 2) & 1;
    kq = idx & 3;
    ars = make_rs((const char*)act + ((size_t)b * 9 * 128 + kq * 32) * 1024, 9 * 128 * 1024 - kq * 32 * 1024);
    wrs = make_rs(wgt + (size_t)kq * 1024 * DM + sel * 256, 1023 * DM * 2 + 512);
    avo = lane * 16;
  }
  const unsigned wvo = srow * WLD + 16 * (sslot ^ (srow << 2));
  auto stage = [&](int step, unsigned slot_addr) {
#pragma unroll
    for (int n = 0; n < 3; ++n) {
      int pc = w + 8 * n;
      pc = pc > 17 ? 17 : pc;
      const int rt = pc >> 1, s = pc & 1;
      const unsigned so = MODE == 0 ? rt * 16 * (DM * 2) + (64 * step + 32 * s) * 2 : (rt * 128 + 2 * step + s) * 1024;
      dma16(ars, avo, so, slot_addr + pc * 1024);
    }
#pragma unroll
    for (int n = 0; n < 4; ++n) {
      const int pc = w + 8 * n;
      dma16(wrs, wvo, (64 * step + 4 * (pc & 15)) * WLD + (pc >> 4) * 256, slot_addr + 18 * 1024 + pc * 1024);
    }
  };
  int woff[2];
#pragma unroll
  for (int t = 0; t < 2; ++t) {
    if (MODE == 0) woff[t] = 18 * 1024 + chf * TILE_BYTES + 256 * (8 * g + q4) + 64 * (u ^ q4) + 16 * p4 + 8 * t;
    else woff[t] = 18 * 1024 + chf * TILE_BYTES + 256 * (8 * g + q4) + 32 * ((2 * u + t) ^ (2 * q4)) + 8 * p4;
  }
  const int aoff = lane * 16;

  f32x4 acc[9][2];
#pragma unroll
  for (int i = 0; i < 9; ++i)
#pragma unroll
    for (int t = 0; t < 2; ++t) acc[i][t] = f32x4{0.f, 0.f, 0.f, 0.f};

  stage(0, lds0);
  stage(1, lds0 + GSLOT);
  int slot = 0;
#pragma unroll 1
  for (int step = 0; step < NSTEP; ++step) {
    if (step + 1 < NSTEP) { WAIT_VM(7); } else { WAIT_VM(0); }
    __builtin_amdgcn_s_barrier();
    const int nslot = slot == 0 ? 2 : slot - 1;
    if (step + 2 < NSTEP) stage(step + 2, lds0 + nslot * GSLOT);
    else asm volatile("" ::: "memory");
    const char* cur = smem + slot * GSLOT;
    bf16x8 wf[2][2];
#pragma unroll
    for (int s = 0; s < 2; ++s)
#pragma unroll
      for (int t = 0; t < 2; ++t)
        wf[s][t] = cat8(tr_read(cur, woff[t] + 8192 * s), tr_read(cur, woff[t] + 8192 * s + 1024));
    auto lda = [&](int n) { return *reinterpret_cast<const bf16x8*>(cur + aoff + (2 * (n % 9) + n / 9) * 1024); };
    bf16x8 fr[4];
    fr[0] = lda(0); fr[1] = lda(1); fr[2] = lda(2);
#pragma unroll
    for (int n = 0; n < 18; ++n) {
      if (n + 3 < 18) fr[(n + 3) & 3] = lda(n + 3);
      SB();
      const int s = n / 9, rt = n % 9;
      acc[rt][0] = MFMA16(wf[s][0], fr[n & 3], acc[rt][0]);
      acc[rt][1] = MFMA16(wf[s][1], fr[n & 3], acc[rt][1]);
      SB();
    }
    slot = slot == 2 ? 0 : slot + 1;
  }

  if (MODE == 0) {
    char* qb = (char*)dst + ((size_t)(b * NH + 2 * sel + chf) * 36 + u) * 1024 + lane * 16;
#pragma unroll
    for (int rt = 0; rt < 9; ++rt) *reinterpret_cast<bf16x8*>(qb + rt * 4096) = pack8_c(acc[rt][0], acc[rt][1]);
  } else {
    const int tile = b * 2 + sel;
    gu32* ticket = (gu32*)(flags + 2 * tile);
    gu32* ready = (gu32*)(flags + 2 * tile + 1);
    unsigned* sflag = (unsigned*)(smem + 3 * GSLOT);
    if (tid == 0) *sflag = __hip_atomic_fetch_add(ticket, 1u, __ATOMIC_RELAXED, __HIP_MEMORY_SCOPE_AGENT);
    __syncthreads();
    const bool last = (*sflag == 3);
    float* pbase = part + (size_t)tile * 4 * (SQ * 256);
    if (!last) {
      const __amdgpu_buffer_rsrc_t prs = __builtin_amdgcn_make_buffer_rsrc(pbase + kq * (SQ * 256), (short)0, SQ * 256 * 4, 0x00020000);
#pragma unroll
      for (int i = 0; i < 9; ++i)
#pragma unroll
        for (int t = 0; t < 2; ++t)
          __builtin_amdgcn_raw_buffer_store_b128(__builtin_bit_cast(u32x4, acc[i][t]), prs,
                                                 ((w * 18 + i * 2 + t) * 64 + lane) * 16, 0, 16);
      asm volatile("s_waitcnt vmcnt(0)" ::: "memory");
      __syncthreads();
      if (tid == 0) __hip_atomic_fetch_add(ready, 1u, __ATOMIC_RELAXED, __HIP_MEMORY_SCOPE_AGENT);
    } else {
      if (tid == 0) {
        while (__hip_atomic_load(ready, __ATOMIC_RELAXED, __HIP_MEMORY_SCOPE_AGENT) != 3) __builtin_amdgcn_s_sleep(2);
        __builtin_amdgcn_fence(__ATOMIC_ACQUIRE, "agent");
        asm volatile("s_waitcnt vmcnt(0)" ::: "memory");
      }
      __syncthreads();
      __hip_bfloat16* out = (__hip_bfloat16*)dst;
#pragma unroll
      for (int i = 0; i < 9; ++i) {
        __hip_bfloat16* orow = out + ((size_t)b * SQ + 16 * i + l15) * DM + sel * 256 + 32 * w + 4 * g;
#pragma unroll
        for (int t = 0; t < 2; ++t) {
          f32x4 v4 = {0.f, 0.f, 0.f, 0.f};
#pragma unroll
          for (int q = 0; q < 4; ++q) {
            f32x4 o = acc[i][t];
            if (q != kq) o = *reinterpret_cast<const f32x4*>(pbase + q * (SQ * 256) + ((w * 18 + i * 2 + t) * 64 + lane) * 4);
            v4 += o;
          }
          u32x2 v = {pack_bf16_c(v4[0], v4[1]), pack_bf16_c(v4[2], v4[3])};
          *reinterpret_cast<u32x2*>(orow + 16 * t) = v;
        }
      }
    }
  }
}

constexpr int NITEM = 2;

__global__ __launch_bounds__(256, 2) void attn_kernel(const __hip_bfloat16* __restrict__ x,
                                                      const __hip_bfloat16* __restrict__ kc,
                                                      const __hip_bfloat16* __restrict__ vc,
                                                      const __hip_bfloat16* __restrict__ wq,
                                                      __hip_bfloat16* __restrict__ obuf,
                                                      unsigned* __restrict__ flags) {
  __shared__ __attribute__((aligned(16))) char smem[4 * TILE_BYTES];
  const int tid = threadIdx.x;
  const int w = __builtin_amdgcn_readfirstlane(tid >> 6);
  const int lane = tid & 63;
  const int g = lane >> 4, l15 = lane & 15, q4 = l15 >> 2, p4 = lane & 3;
  const unsigned lds0 = (unsigned)(size_t)(__attribute__((address_space(3))) char*)smem;
  if (blockIdx.x == 0) flags[tid] = 0;

  if (w == 3) {
    const int srow = lane >> 4, sslot = lane & 15;
    const unsigned kvo = (lane >> 3) * 256 + 16 * ((lane & 7) ^ (((lane >> 4) & 3) << 1));
    const unsigned wvo = srow * (HD * 2) + 16 * (sslot ^ (srow << 2));
    auto stage = [&](int n) {
      const int it = n >= 24 ? 1 : 0, t = n - 24 * it;
      const int bh = blockIdx.x + 512 * it;
      const unsigned slot_addr = lds0 + (n & 3) * TILE_BYTES;
      if (t < 8) {
        const u32x4 wrs = make_rs(wq + (bh & 31) * DH, (DM - 1) * HD * 2 + DH * 2);
#pragma unroll
        for (int pp = 0; pp < 16; ++pp) dma16(wrs, wvo, (64 * t + 4 * pp) * (HD * 2), slot_addr + pp * 1024);
      } else {
        const int j = (t - 8) >> 1;
        const u32x4 rs = make_rs(((t & 1) ? vc : kc) + (size_t)bh * SK * DH, SK * DH * 2);
#pragma unroll
        for (int pp = 0; pp < 16; ++pp)
          dma16_nt(rs, kvo, (64 * j + 8 * (pp >> 1)) * 256 + (pp & 1) * 128, slot_addr + pp * 1024);
      }
    };
    stage(0); stage(1); stage(2);
#pragma unroll 1
    for (int n = 0; n < 24 * NITEM - 3; ++n) {
      WAIT_VM(32);
      __builtin_amdgcn_s_barrier();
      stage(n + 3);
    }
    WAIT_VM(32); __builtin_amdgcn_s_barrier();
    WAIT_VM(16); __builtin_amdgcn_s_barrier();
    WAIT_VM(0);  __builtin_amdgcn_s_barrier();
    return;
  }

  int woff[4];
#pragma unroll
  for (int u = 0; u < 4; ++u) woff[u] = 256 * (8 * g + q4) + 64 * (u ^ q4) + 16 * p4;
  int koff[4];
#pragma unroll
  for (int u = 0; u < 4; ++u)
    koff[u] = (l15 >> 3) * 2048 + (u >> 1) * 1024 + (l15 & 7) * 128 + 16 * ((4 * (u & 1) + g) ^ (((l15 >> 1) & 3) << 1));
  int voff[4];
  {
    const int pr = 2 * (g & 1) + (q4 >> 1);
#pragma unroll
    for (int c = 0; c < 4; ++c)
      voff[c] = TILE_BYTES + (g >> 1) * 2048 + (4 * (g & 1) + q4) * 128 + 32 * (c ^ pr) + 8 * p4;
  }
  constexpr float CSC = 0.08838834764831845f * 1.4426950408889634f;
#define SYNC() do { asm volatile("s_waitcnt lgkmcnt(0)" ::: "memory"); __builtin_amdgcn_s_barrier(); asm volatile("" ::: "memory"); } while (0)

#pragma unroll 1
  for (int it = 0; it < NITEM; ++it) {
    const int bh = blockIdx.x + 512 * it, b = bh >> 5, h = bh & 31;
    bf16x8 qf[3][4];
    {
      const __hip_bfloat16* xbase = x + ((size_t)b * SQ + 48 * w + l15) * DM + 8 * g;
      f32x4 qacc[3][8];
#pragma unroll
      for (int i = 0; i < 3; ++i)
#pragma unroll
        for (int c = 0; c < 8; ++c) qacc[i][c] = f32x4{0.f, 0.f, 0.f, 0.f};
      bf16x8 xf[6];
#pragma unroll
      for (int n = 0; n < 6; ++n) xf[n] = *reinterpret_cast<const bf16x8*>(xbase + (size_t)16 * (n >> 1) * DM + 32 * (n & 1));
#pragma unroll 1
      for (int step = 0; step < 8; ++step) {
        SYNC();
        bf16x8 xn[6];
        const int sn = step < 7 ? step + 1 : 7;
#pragma unroll
        for (int n = 0; n < 6; ++n)
          xn[n] = *reinterpret_cast<const bf16x8*>(xbase + (size_t)16 * (n >> 1) * DM + 64 * sn + 32 * (n & 1));
        const char* wcur = smem + (step & 3) * TILE_BYTES;
        auto ldw = [&](int n) {
          const int s = n >> 3, u = (n >> 1) & 3, t = n & 1;
          const s16x4 lo = tr_read(wcur, woff[u] + 8192 * s + 8 * t);
          const s16x4 hi = tr_read(wcur, woff[u] + 8192 * s + 1024 + 8 * t);
          return cat8(lo, hi);
        };
        bf16x8 fr[4];
        fr[0] = ldw(0); fr[1] = ldw(1); fr[2] = ldw(2);
#pragma unroll
        for (int n = 0; n < 16; ++n) {
          if (n + 3 < 16) fr[(n + 3) & 3] = ldw(n + 3);
          SB();
          const int s = n >> 3, c = n & 7;
#pragma unroll
          for (int i = 0; i < 3; ++i) qacc[i][c] = MFMA16(fr[n & 3], xf[2 * i + s], qacc[i][c]);
          SB();
        }
#pragma unroll
        for (int n = 0; n < 6; ++n) xf[n] = xn[n];
      }
#pragma unroll
      for (int i = 0; i < 3; ++i)
#pragma unroll
        for (int u = 0; u < 4; ++u) qf[i][u] = pack8_c(qacc[i][2 * u], qacc[i][2 * u + 1]);
    }

    f32x4 oacc[3][8];
#pragma unroll
    for (int i = 0; i < 3; ++i)
#pragma unroll
      for (int c = 0; c < 8; ++c) oacc[i][c] = f32x4{0.f, 0.f, 0.f, 0.f};
    float mrow[3], lsum[3];
#pragma unroll
    for (int i = 0; i < 3; ++i) { mrow[i] = -__builtin_inff(); lsum[i] = 0.f; }

#pragma unroll 1
    for (int j = 0; j < 8; ++j) {
      const char* kvb = smem + (j & 1) * 2 * TILE_BYTES;
      SYNC();
      f32x4 sacc[4][3];
#pragma unroll
      for (int kt = 0; kt < 4; ++kt)
#pragma unroll
        for (int i = 0; i < 3; ++i) sacc[kt][i] = f32x4{0.f, 0.f, 0.f, 0.f};
      {
        auto ldk = [&](int n) { return *reinterpret_cast<const bf16x8*>(kvb + koff[n & 3] + 4096 * (n >> 2)); };
        bf16x8 fr[4];
        fr[0] = ldk(0); fr[1] = ldk(1); fr[2] = ldk(2);
#pragma unroll
        for (int n = 0; n < 16; ++n) {
          if (n + 3 < 16) fr[(n + 3) & 3] = ldk(n + 3);
          SB();
          const int kt = n >> 2, u = n & 3;
#pragma unroll
          for (int i = 0; i < 3; ++i) sacc[kt][i] = MFMA16(fr[n & 3], qf[i][u], sacc[kt][i]);
          SB();
        }
      }
      bf16x8 pf[3][2];
#pragma unroll
      for (int i = 0; i < 3; ++i) {
        float mx = sacc[0][i][0];
#pragma unroll
        for (int kt = 0; kt < 4; ++kt)
#pragma unroll
          for (int r = 0; r < 4; ++r) mx = fmaxf(mx, sacc[kt][i][r]);
        mx = grp_max(mx);
        const float mnew = fmaxf(mrow[i], mx);
        const float alpha = __builtin_amdgcn_exp2f((mrow[i] - mnew) * CSC);
        mrow[i] = mnew;
        const float mc = -mnew * CSC;
        float ps = 0.f;
#pragma unroll
        for (int kt = 0; kt < 4; ++kt)
#pragma unroll
          for (int r = 0; r < 4; ++r) {
            const float pv = __builtin_amdgcn_exp2f(fmaf(sacc[kt][i][r], CSC, mc));
            sacc[kt][i][r] = pv;
            ps += pv;
          }
        lsum[i] = lsum[i] * alpha + ps;
#pragma unroll
        for (int c = 0; c < 8; ++c) oacc[i][c] *= alpha;
        pf[i][0] = pack8(sacc[0][i], sacc[1][i]);
        pf[i][1] = pack8(sacc[2][i], sacc[3][i]);
      }
      SYNC();
      {
        auto ldv = [&](int n) {
          const int ks = n >> 3, c = n & 7;
          const s16x4 lo = tr_read(kvb, voff[c & 3] + (c >> 2) * 1024 + 8192 * ks);
          const s16x4 hi = tr_read(kvb, voff[c & 3] + (c >> 2) * 1024 + 8192 * ks + 4096);
          return cat8(lo, hi);
        };
        bf16x8 fr[4];
        fr[0] = ldv(0); fr[1] = ldv(1); fr[2] = ldv(2);
#pragma unroll
        for (int n = 0; n < 16; ++n) {
          if (n + 3 < 16) fr[(n + 3) & 3] = ldv(n + 3);
          SB();
          const int ks = n >> 3, c = n & 7;
#pragma unroll
          for (int i = 0; i < 3; ++i) oacc[i][c] = MFMA16(fr[n & 3], pf[i][ks], oacc[i][c]);
          SB();
        }
      }
    }

    int lane2 = __builtin_amdgcn_mbcnt_hi(~0u, __builtin_amdgcn_mbcnt_lo(~0u, 0u));
    asm volatile("" : "+v"(lane2));
#pragma unroll
    for (int i = 0; i < 3; ++i) {
      const float l = grp_sum(lsum[i]);
      const float inv = 1.0f / l;
      const int g2 = lane2 >> 4;
      char* obase = (char*)obuf + ((size_t)(b * 9 + 3 * w + i) * 128 + 4 * h) * 1024 +
                    (((g2 >> 1) * 16 + (lane2 & 15)) * 16 + 8 * (g2 & 1));
#pragma unroll
      for (int c = 0; c < 8; ++c) {
        u32x2 v = {pack_bf16(oacc[i][c][0] * inv, oacc[i][c][1] * inv), pack_bf16(oacc[i][c][2] * inv, oacc[i][c][3] * inv)};
        *reinterpret_cast<u32x2*>(obase + (c >> 1) * 1024 + (c & 1) * 512) = v;
      }
    }
  }
#undef SYNC
}

extern "C" void kernel_launch(void* const* d_in, const int* in_sizes, int n_in, void* d_out, int out_size,
                              void* d_ws, size_t ws_size, hipStream_t stream) {
  const __hip_bfloat16* x = (const __hip_bfloat16*)d_in[0];
  const __hip_bfloat16* kc = (const __hip_bfloat16*)d_in[1];
  const __hip_bfloat16* vc = (const __hip_bfloat16*)d_in[2];
  const __hip_bfloat16* wq = (const __hip_bfloat16*)d_in[3];
  const __hip_bfloat16* wo = (const __hip_bfloat16*)d_in[4];
  char* ws = (char*)d_ws;
  char* qbuf = ws;
  char* obuf = ws + (40u << 20);
  float* part = (float*)(ws + (80u << 20));
  unsigned* flags = (unsigned*)(ws + (128u << 20));
  attn_kernel<<<512, 256, 0, stream>>>(x, kc, vc, wq, (__hip_bfloat16*)obuf, flags);
  proj_kernel<1><<<256, 512, 0, stream>>>(obuf, wo, d_out, part, flags);
}
```

```cpp
#include <hip/hip_runtime.h>
#include <hip/hip_bf16.h>

typedef __attribute__((ext_vector_type(8))) short bf16x8;
typedef __attribute__((ext_vector_type(4))) short s16x4;
typedef __attribute__((ext_vector_type(4))) float f32x4;
typedef __attribute__((ext_vector_type(2))) unsigned u32x2;
typedef __attribute__((ext_vector_type(4))) unsigned u32x4;
typedef __attribute__((address_space(3))) s16x4* lds_s16x4_ptr;
typedef __attribute__((address_space(3))) void* lds_void_ptr;

constexpr int NB = 32, SQ = 144, DM = 512, NH = 32, SK = 512, DH = 128;
constexpr int HD = NH * DH;

#define MFMA16(a, b, c) __builtin_amdgcn_mfma_f32_16x16x32_bf16((a), (b), (c), 0, 0, 0)

__device__ __forceinline__ unsigned pack_bf16(float lo, float hi) {
  unsigned r;
  asm("v_cvt_pk_bf16_f32 %0, %1, %2" : "=v"(r) : "v"(lo), "v"(hi));
  return r;
}
__device__ __forceinline__ float grp_max(float x) {
  auto r = __builtin_amdgcn_permlane16_swap(__float_as_uint(x), __float_as_uint(x), false, false);
  x = fmaxf(__uint_as_float(r[0]), __uint_as_float(r[1]));
  auto q = __builtin_amdgcn_permlane32_swap(__float_as_uint(x), __float_as_uint(x), false, false);
  return fmaxf(__uint_as_float(q[0]), __uint_as_float(q[1]));
}
__device__ __forceinline__ float grp_sum(float x) {
  auto r = __builtin_amdgcn_permlane16_swap(__float_as_uint(x), __float_as_uint(x), false, false);
  x = __uint_as_float(r[0]) + __uint_as_float(r[1]);
  auto q = __builtin_amdgcn_permlane32_swap(__float_as_uint(x), __float_as_uint(x), false, false);
  return __uint_as_float(q[0]) + __uint_as_float(q[1]);
}
__device__ __forceinline__ unsigned pack_bf16_c(float lo, float hi) {
  __hip_bfloat16 a = __float2bfloat16(lo), b = __float2bfloat16(hi);
  unsigned short ua = *reinterpret_cast<unsigned short*>(&a), ub = *reinterpret_cast<unsigned short*>(&b);
  return (unsigned)ua | ((unsigned)ub << 16);
}
__device__ __forceinline__ bf16x8 pack8_c(const f32x4& a, const f32x4& b) {
  u32x4 w = {pack_bf16_c(a[0], a[1]), pack_bf16_c(a[2], a[3]), pack_bf16_c(b[0], b[1]), pack_bf16_c(b[2], b[3])};
  return __builtin_bit_cast(bf16x8, w);
}
__device__ __forceinline__ bf16x8 pack8(const f32x4& a, const f32x4& b) {
  u32x4 w = {pack_bf16(a[0], a[1]), pack_bf16(a[2], a[3]), pack_bf16(b[0], b[1]), pack_bf16(b[2], b[3])};
  return __builtin_bit_cast(bf16x8, w);
}
__device__ __forceinline__ s16x4 tr_read(const char* smem, int byte_off) {
  return __builtin_amdgcn_ds_read_tr16_b64_v4i16((lds_s16x4_ptr)(smem + byte_off));
}
__device__ __forceinline__ bf16x8 cat8(const s16x4& lo, const s16x4& hi) {
  return __builtin_shufflevector(lo, hi, 0, 1, 2, 3, 4, 5, 6, 7);
}
__device__ __forceinline__ void glds16(const void* gsrc, char* lds_dst) {
  __builtin_amdgcn_global_load_lds(gsrc, (lds_void_ptr)lds_dst, 16, 0, 0);
}
typedef __amdgpu_buffer_rsrc_t rsrc_t;
__device__ __forceinline__ rsrc_t make_rsrc(const void* base, unsigned bytes) {
  return __builtin_amdgcn_make_buffer_rsrc(const_cast<void*>(base), (short)0, bytes, 0x00020000);
}
__device__ __forceinline__ void blds16(rsrc_t r, unsigned voff, unsigned soff, char* lds_dst) {
  __builtin_amdgcn_raw_ptr_buffer_load_lds(r, (lds_void_ptr)lds_dst, 16, voff, soff, 0, 0);
}

constexpr int TILE_BYTES = 64 * 256;

__device__ __forceinline__ u32x4 make_rs(const void* base, unsigned bytes) {
  const unsigned long long a = (unsigned long long)base;
  u32x4 r = {(unsigned)a, (unsigned)(a >> 32) & 0xffffu, bytes, 0x00020000u};
  return r;
}
__device__ __forceinline__ void dma16(const u32x4& rs, unsigned voff, unsigned soff, unsigned lds_addr) {
  asm volatile("s_mov_b32 m0, %0\n\ts_nop 4\n\tbuffer_load_dwordx4 %1, %2, %3 offen lds"
               :: "s"(__builtin_amdgcn_readfirstlane(lds_addr)), "v"(voff), "s"(rs), "s"(__builtin_amdgcn_readfirstlane(soff)) : "memory");
}
__device__ __forceinline__ void dma16_nt(const u32x4& rs, unsigned voff, unsigned soff, unsigned lds_addr) {
  asm volatile("s_mov_b32 m0, %0\n\ts_nop 4\n\tbuffer_load_dwordx4 %1, %2, %3 offen nt lds"
               :: "s"(__builtin_amdgcn_readfirstlane(lds_addr)), "v"(voff), "s"(rs), "s"(__builtin_amdgcn_readfirstlane(soff)) : "memory");
}
#define SB() __builtin_amdgcn_sched_barrier(0)
#define WAIT_VM(n) asm volatile("s_waitcnt vmcnt(" #n ")" ::: "memory")
typedef __attribute__((address_space(1))) unsigned gu32;

constexpr int GSLOT = 18 * 1024 + 2 * TILE_BYTES;

template <int MODE>
__global__ __launch_bounds__(768, 1) void proj_kernel(const void* __restrict__ act, const __hip_bfloat16* __restrict__ wgt,
                                                      void* __restrict__ dst, float* __restrict__ part,
                                                      unsigned* __restrict__ flags) {
  __shared__ __attribute__((aligned(16))) char smem[3 * GSLOT + 16];
  const int tid = threadIdx.x;
  const int w = __builtin_amdgcn_readfirstlane(tid >> 6);
  const int chf = w >> 2, u = w & 3;
  const int lane = tid & 63;
  const int g = lane >> 4, l15 = lane & 15, q4 = l15 >> 2, p4 = lane & 3;
  const int srow = lane >> 4, sslot = lane & 15;
  const unsigned lds0 = (unsigned)(size_t)(__attribute__((address_space(3))) char*)smem;
  constexpr int NSTEP = MODE == 0 ? 8 : 16;
  constexpr int WLD = MODE == 0 ? HD * 2 : DM * 2;

  const int idx = blockIdx.x >> 3;
  int b, sel, kq = 0;
  u32x4 ars, wrs;
  unsigned avo;
  if (MODE == 0) {
    if (blockIdx.x == 0 && tid < 256) flags[tid] = 0;
    b = (blockIdx.x & 7) * 4 + (idx >> 4);
    sel = idx & 15;
    ars = make_rs((const char*)act + (size_t)b * SQ * DM * 2, SQ * DM * 2);
    wrs = make_rs(wgt + sel * 256, (DM - 1) * HD * 2 + 512);
    avo = l15 * (DM * 2) + g * 16;
  } else {
    b = (blockIdx.x & 7) * 4 + (idx >> 3);
    sel = (idx >> 2) & 1;
    kq = idx & 3;
    ars = make_rs((const char*)act + ((size_t)b * 9 * 128 + kq * 32) * 1024, 9 * 128 * 1024 - kq * 32 * 1024);
    wrs = make_rs(wgt + (size_t)kq * 1024 * DM + sel * 256, 1023 * DM * 2 + 512);
    avo = lane * 16;
  }
  const unsigned wvo = srow * WLD + 16 * (sslot ^ (srow << 2));
  if (w >= 8) {
    const int lw = w - 8;
    __builtin_amdgcn_s_setprio(3);
    auto stage = [&](int step, unsigned slot_addr) {
#pragma unroll
      for (int n = 0; n < 5; ++n) {
        int pc = lw + 4 * n;
        pc = pc > 17 ? 17 : pc;
        const int rt = pc >> 1, s = pc & 1;
        const unsigned so = MODE == 0 ? rt * 16 * (DM * 2) + (64 * step + 32 * s) * 2 : (rt * 128 + 2 * step + s) * 1024;
        dma16_nt(ars, avo, so, slot_addr + pc * 1024);
      }
#pragma unroll
      for (int n = 0; n < 8; ++n) {
        const int pc = lw + 4 * n;
        dma16(wrs, wvo, (64 * step + 4 * (pc & 15)) * WLD + (pc >> 4) * 256, slot_addr + 18 * 1024 + pc * 1024);
      }
    };
    stage(0, lds0);
    stage(1, lds0 + GSLOT);
    int slot = 0;
#pragma unroll 1
    for (int step = 0; step < NSTEP; ++step) {
      if (step + 1 < NSTEP) { WAIT_VM(13); } else { WAIT_VM(0); }
      __builtin_amdgcn_s_barrier();
      const int nslot = slot == 0 ? 2 : slot - 1;
      if (step + 2 < NSTEP) stage(step + 2, lds0 + nslot * GSLOT);
      slot = slot == 2 ? 0 : slot + 1;
    }
    if (MODE == 1) { __builtin_amdgcn_s_barrier(); __builtin_amdgcn_s_barrier(); }
    return;
  }
  int woff[2];
#pragma unroll
  for (int t = 0; t < 2; ++t) {
    if (MODE == 0) woff[t] = 18 * 1024 + chf * TILE_BYTES + 256 * (8 * g + q4) + 64 * (u ^ q4) + 16 * p4 + 8 * t;
    else woff[t] = 18 * 1024 + chf * TILE_BYTES + 256 * (8 * g + q4) + 32 * ((2 * u + t) ^ (2 * q4)) + 8 * p4;
  }
  const int aoff = lane * 16;

  f32x4 acc[9][2];
#pragma unroll
  for (int i = 0; i < 9; ++i)
#pragma unroll
    for (int t = 0; t < 2; ++t) acc[i][t] = f32x4{0.f, 0.f, 0.f, 0.f};

  int slot = 0;
#pragma unroll 1
  for (int step = 0; step < NSTEP; ++step) {
    asm volatile("s_waitcnt lgkmcnt(0)" ::: "memory");
    __builtin_amdgcn_s_barrier();
    asm volatile("" ::: "memory");
    const char* cur = smem + slot * GSLOT;
    bf16x8 wf[2][2];
#pragma unroll
    for (int s = 0; s < 2; ++s)
#pragma unroll
      for (int t = 0; t < 2; ++t)
        wf[s][t] = cat8(tr_read(cur, woff[t] + 8192 * s), tr_read(cur, woff[t] + 8192 * s + 1024));
    auto lda = [&](int n) { return *reinterpret_cast<const bf16x8*>(cur + aoff + (2 * (n % 9) + n / 9) * 1024); };
    bf16x8 fr[4];
    fr[0] = lda(0); fr[1] = lda(1); fr[2] = lda(2);
#pragma unroll
    for (int n = 0; n < 18; ++n) {
      if (n + 3 < 18) fr[(n + 3) & 3] = lda(n + 3);
      SB();
      const int s = n / 9, rt = n % 9;
      acc[rt][0] = MFMA16(wf[s][0], fr[n & 3], acc[rt][0]);
      acc[rt][1] = MFMA16(wf[s][1], fr[n & 3], acc[rt][1]);
      SB();
    }
    slot = slot == 2 ? 0 : slot + 1;
  }

  if (MODE == 0) {
    char* qb = (char*)dst + ((size_t)(b * NH + 2 * sel + chf) * 36 + u) * 1024 + lane * 16;
#pragma unroll
    for (int rt = 0; rt < 9; ++rt) *reinterpret_cast<bf16x8*>(qb + rt * 4096) = pack8_c(acc[rt][0], acc[rt][1]);
  } else {
    const int tile = b * 2 + sel;
    gu32* ready = (gu32*)(flags + tile);
    float* pbase = part + (size_t)tile * 4 * (SQ * 256);
    {
      const __amdgpu_buffer_rsrc_t prs = __builtin_amdgcn_make_buffer_rsrc(pbase + kq * (SQ * 256), (short)0, SQ * 256 * 4, 0x00020000);
#pragma unroll
      for (int e = 0; e < 18; ++e)
        if ((e & 3) != kq)
          __builtin_amdgcn_raw_buffer_store_b128(__builtin_bit_cast(u32x4, acc[e >> 1][e & 1]), prs,
                                                 ((w * 18 + e) * 64 + lane) * 16, 0, 16);
    }
    asm volatile("s_waitcnt vmcnt(0)" ::: "memory");
    __syncthreads();
    if (tid == 0) {
      __hip_atomic_fetch_add(ready, 1u, __ATOMIC_RELAXED, __HIP_MEMORY_SCOPE_AGENT);
      while (__hip_atomic_load(ready, __ATOMIC_RELAXED, __HIP_MEMORY_SCOPE_AGENT) != 4) __builtin_amdgcn_s_sleep(2);
      __builtin_amdgcn_fence(__ATOMIC_ACQUIRE, "agent");
      asm volatile("s_waitcnt vmcnt(0)" ::: "memory");
    }
    __syncthreads();
    __hip_bfloat16* out = (__hip_bfloat16*)dst;
#pragma unroll
    for (int e = 0; e < 18; ++e) {
      if ((e & 3) == kq) {
        const int i = e >> 1, t = e & 1;
        f32x4 v4 = {0.f, 0.f, 0.f, 0.f};
#pragma unroll
        for (int q = 0; q < 4; ++q) {
          f32x4 o = acc[i][t];
          if (q != kq) o = *reinterpret_cast<const f32x4*>(pbase + q * (SQ * 256) + ((w * 18 + e) * 64 + lane) * 4);
          v4 += o;
        }
        __hip_bfloat16* orow = out + ((size_t)b * SQ + 16 * i + l15) * DM + sel * 256 + 32 * w + 4 * g + 16 * t;
        u32x2 v = {pack_bf16_c(v4[0], v4[1]), pack_bf16_c(v4[2], v4[3])};
        *reinterpret_cast<u32x2*>(orow) = v;
      }
    }
  }
}

constexpr int NITEM = 2;

__global__ __launch_bounds__(256, 2) void attn_kernel(const __hip_bfloat16* __restrict__ x,
                                                      const __hip_bfloat16* __restrict__ kc,
                                                      const __hip_bfloat16* __restrict__ vc,
                                                      const __hip_bfloat16* __restrict__ wq,
                                                      __hip_bfloat16* __restrict__ obuf,
                                                      unsigned* __restrict__ flags) {
  __shared__ __attribute__((aligned(16))) char smem[4 * TILE_BYTES];
  const int tid = threadIdx.x;
  const int w = __builtin_amdgcn_readfirstlane(tid >> 6);
  const int lane = tid & 63;
  const int g = lane >> 4, l15 = lane & 15, q4 = l15 >> 2, p4 = lane & 3;
  const unsigned lds0 = (unsigned)(size_t)(__attribute__((address_space(3))) char*)smem;
  if (blockIdx.x == 0) flags[tid] = 0;

  if (w == 3) {
    __builtin_amdgcn_s_setprio(3);
    const int srow = lane >> 4, sslot = lane & 15;
    const unsigned kvo = (lane >> 3) * 256 + 16 * ((lane & 7) ^ (((lane >> 4) & 3) << 1));
    const unsigned wvo = srow * (HD * 2) + 16 * (sslot ^ (srow << 2));
    auto stage = [&](int n) {
      const int it = n >= 24 ? 1 : 0, t = n - 24 * it;
      const int bh = blockIdx.x + 512 * it;
      const unsigned slot_addr = lds0 + (n & 3) * TILE_BYTES;
      if (t < 8) {
        const u32x4 wrs = make_rs(wq + (bh & 31) * DH, (DM - 1) * HD * 2 + DH * 2);
#pragma unroll
        for (int pp = 0; pp < 16; ++pp) dma16(wrs, wvo, (64 * t + 4 * pp) * (HD * 2), slot_addr + pp * 1024);
      } else {
        const int j = (t - 8) >> 1;
        const u32x4 rs = make_rs(((t & 1) ? vc : kc) + (size_t)bh * SK * DH, SK * DH * 2);
#pragma unroll
        for (int pp = 0; pp < 16; ++pp)
          dma16_nt(rs, kvo, (64 * j + 8 * (pp >> 1)) * 256 + (pp & 1) * 128, slot_addr + pp * 1024);
      }
    };
    stage(0); stage(1); stage(2);
#pragma unroll 1
    for (int n = 0; n < 24 * NITEM - 3; ++n) {
      WAIT_VM(32);
      __builtin_amdgcn_s_barrier();
      stage(n + 3);
    }
    WAIT_VM(32); __builtin_amdgcn_s_barrier();
    WAIT_VM(16); __builtin_amdgcn_s_barrier();
    WAIT_VM(0);  __builtin_amdgcn_s_barrier();
    return;
  }

  int woff[4];
#pragma unroll
  for (int u = 0; u < 4; ++u) woff[u] = 256 * (8 * g + q4) + 64 * (u ^ q4) + 16 * p4;
  int koff[4];
#pragma unroll
  for (int u = 0; u < 4; ++u)
    koff[u] = (l15 >> 3) * 2048 + (u >> 1) * 1024 + (l15 & 7) * 128 + 16 * ((4 * (u & 1) + g) ^ (((l15 >> 1) & 3) << 1));
  int voff[4];
  {
    const int pr = 2 * (g & 1) + (q4 >> 1);
#pragma unroll
    for (int c = 0; c < 4; ++c)
      voff[c] = TILE_BYTES + (g >> 1) * 2048 + (4 * (g & 1) + q4) * 128 + 32 * (c ^ pr) + 8 * p4;
  }
  constexpr float CSC = 0.08838834764831845f * 1.4426950408889634f;
#define SYNC() do { asm volatile("s_waitcnt lgkmcnt(0)" ::: "memory"); __builtin_amdgcn_s_barrier(); asm volatile("" ::: "memory"); } while (0)

#pragma unroll 1
  for (int it = 0; it < NITEM; ++it) {
    const int bh = blockIdx.x + 512 * it, b = bh >> 5, h = bh & 31;
    bf16x8 qf[3][4];
    {
      const __hip_bfloat16* xbase = x + ((size_t)b * SQ + 48 * w + l15) * DM + 8 * g;
      f32x4 qacc[3][8];
#pragma unroll
      for (int i = 0; i < 3; ++i)
#pragma unroll
        for (int c = 0; c < 8; ++c) qacc[i][c] = f32x4{0.f, 0.f, 0.f, 0.f};
      bf16x8 xf[6];
#pragma unroll
      for (int n = 0; n < 6; ++n) xf[n] = *reinterpret_cast<const bf16x8*>(xbase + (size_t)16 * (n >> 1) * DM + 32 * (n & 1));
#pragma unroll 1
      for (int step = 0; step < 8; ++step) {
        SYNC();
        bf16x8 xn[6];
        const int sn = step < 7 ? step + 1 : 7;
#pragma unroll
        for (int n = 0; n < 6; ++n)
          xn[n] = *reinterpret_cast<const bf16x8*>(xbase + (size_t)16 * (n >> 1) * DM + 64 * sn + 32 * (n & 1));
        const char* wcur = smem + (step & 3) * TILE_BYTES;
        auto ldw = [&](int n) {
          const int s = n >> 3, u = (n >> 1) & 3, t = n & 1;
          const s16x4 lo = tr_read(wcur, woff[u] + 8192 * s + 8 * t);
          const s16x4 hi = tr_read(wcur, woff[u] + 8192 * s + 1024 + 8 * t);
          return cat8(lo, hi);
        };
        bf16x8 fr[4];
        fr[0] = ldw(0); fr[1] = ldw(1); fr[2] = ldw(2);
#pragma unroll
        for (int n = 0; n < 16; ++n) {
          if (n + 3 < 16) fr[(n + 3) & 3] = ldw(n + 3);
          SB();
          const int s = n >> 3, c = n & 7;
#pragma unroll
          for (int i = 0; i < 3; ++i) qacc[i][c] = MFMA16(fr[n & 3], xf[2 * i + s], qacc[i][c]);
          SB();
        }
#pragma unroll
        for (int n = 0; n < 6; ++n) xf[n] = xn[n];
      }
#pragma unroll
      for (int i = 0; i < 3; ++i)
#pragma unroll
        for (int u = 0; u < 4; ++u) qf[i][u] = pack8_c(qacc[i][2 * u], qacc[i][2 * u + 1]);
    }

    f32x4 oacc[3][8];
#pragma unroll
    for (int i = 0; i < 3; ++i)
#pragma unroll
      for (int c = 0; c < 8; ++c) oacc[i][c] = f32x4{0.f, 0.f, 0.f, 0.f};
    float mrow[3], lsum[3];
#pragma unroll
    for (int i = 0; i < 3; ++i) { mrow[i] = -__builtin_inff(); lsum[i] = 0.f; }

#pragma unroll 1
    for (int j = 0; j < 8; ++j) {
      const char* kvb = smem + (j & 1) * 2 * TILE_BYTES;
      SYNC();
      f32x4 sacc[4][3];
#pragma unroll
      for (int kt = 0; kt < 4; ++kt)
#pragma unroll
        for (int i = 0; i < 3; ++i) sacc[kt][i] = f32x4{0.f, 0.f, 0.f, 0.f};
      {
        auto ldk = [&](int n) { return *reinterpret_cast<const bf16x8*>(kvb + koff[n & 3] + 4096 * (n >> 2)); };
        bf16x8 fr[4];
        fr[0] = ldk(0); fr[1] = ldk(1); fr[2] = ldk(2);
#pragma unroll
        for (int n = 0; n < 16; ++n) {
          if (n + 3 < 16) fr[(n + 3) & 3] = ldk(n + 3);
          SB();
          const int kt = n >> 2, u = n & 3;
#pragma unroll
          for (int i = 0; i < 3; ++i) sacc[kt][i] = MFMA16(fr[n & 3], qf[i][u], sacc[kt][i]);
          SB();
        }
      }
      bf16x8 pf[3][2];
#pragma unroll
      for (int i = 0; i < 3; ++i) {
        float mx = sacc[0][i][0];
#pragma unroll
        for (int kt = 0; kt < 4; ++kt)
#pragma unroll
          for (int r = 0; r < 4; ++r) mx = fmaxf(mx, sacc[kt][i][r]);
        mx = grp_max(mx);
        const float mnew = fmaxf(mrow[i], mx);
        const float alpha = __builtin_amdgcn_exp2f((mrow[i] - mnew) * CSC);
        mrow[i] = mnew;
        const float mc = -mnew * CSC;
        float ps = 0.f;
#pragma unroll
        for (int kt = 0; kt < 4; ++kt)
#pragma unroll
          for (int r = 0; r < 4; ++r) {
            const float pv = __builtin_amdgcn_exp2f(fmaf(sacc[kt][i][r], CSC, mc));
            sacc[kt][i][r] = pv;
            ps += pv;
          }
        lsum[i] = lsum[i] * alpha + ps;
#pragma unroll
        for (int c = 0; c < 8; ++c) oacc[i][c] *= alpha;
        pf[i][0] = pack8(sacc[0][i], sacc[1][i]);
        pf[i][1] = pack8(sacc[2][i], sacc[3][i]);
      }
      SYNC();
      {
        auto ldv = [&](int n) {
          const int ks = n >> 3, c = n & 7;
          const s16x4 lo = tr_read(kvb, voff[c & 3] + (c >> 2) * 1024 + 8192 * ks);
          const s16x4 hi = tr_read(kvb, voff[c & 3] + (c >> 2) * 1024 + 8192 * ks + 4096);
          return cat8(lo, hi);
        };
        bf16x8 fr[4];
        fr[0] = ldv(0); fr[1] = ldv(1); fr[2] = ldv(2);
#pragma unroll
        for (int n = 0; n < 16; ++n) {
          if (n + 3 < 16) fr[(n + 3) & 3] = ldv(n + 3);
          SB();
          const int ks = n >> 3, c = n & 7;
#pragma unroll
          for (int i = 0; i < 3; ++i) oacc[i][c] = MFMA16(fr[n & 3], pf[i][ks], oacc[i][c]);
          SB();
        }
      }
    }

    int lane2 = __builtin_amdgcn_mbcnt_hi(~0u, __builtin_amdgcn_mbcnt_lo(~0u, 0u));
    asm volatile("" : "+v"(lane2));
#pragma unroll
    for (int i = 0; i < 3; ++i) {
      const float l = grp_sum(lsum[i]);
      const float inv = 1.0f / l;
      const int g2 = lane2 >> 4;
      char* obase = (char*)obuf + ((size_t)(b * 9 + 3 * w + i) * 128 + 4 * h) * 1024 +
                    (((g2 >> 1) * 16 + (lane2 & 15)) * 16 + 8 * (g2 & 1));
#pragma unroll
      for (int c = 0; c < 8; ++c) {
        u32x2 v = {pack_bf16(oacc[i][c][0] * inv, oacc[i][c][1] * inv), pack_bf16(oacc[i][c][2] * inv, oacc[i][c][3] * inv)};
        *reinterpret_cast<u32x2*>(obase + (c >> 1) * 1024 + (c & 1) * 512) = v;
      }
    }
  }
#undef SYNC
}

extern "C" void kernel_launch(void* const* d_in, const int* in_sizes, int n_in, void* d_out, int out_size,
                              void* d_ws, size_t ws_size, hipStream_t stream) {
  const __hip_bfloat16* x = (const __hip_bfloat16*)d_in[0];
  const __hip_bfloat16* kc = (const __hip_bfloat16*)d_in[1];
  const __hip_bfloat16* vc = (const __hip_bfloat16*)d_in[2];
  const __hip_bfloat16* wq = (const __hip_bfloat16*)d_in[3];
  const __hip_bfloat16* wo = (const __hip_bfloat16*)d_in[4];
  char* ws = (char*)d_ws;
  char* obuf = ws + (40u << 20);
  float* part = (float*)(ws + (80u << 20));
  unsigned* flags = (unsigned*)(ws + (128u << 20));
  attn_kernel<<<512, 256, 0, stream>>>(x, kc, vc, wq, (__hip_bfloat16*)obuf, flags);
  proj_kernel<1><<<256, 768, 0, stream>>>(obuf, wo, d_out, part, flags);
}
```
